```python
import torch

def _softmax(x, axis=-1):
    return torch.softmax(x, dim=axis)
def _gelu(x):
    return torch.nn.functional.gelu(x, approximate="tanh")
def _silu(x):
    return torch.nn.functional.silu(x)
def _sigmoid(x):
    return torch.sigmoid(x)
def _layernorm(x, axis=-1, eps=1e-5):
    m = x.mean(dim=axis, keepdim=True)
    v = x.var(dim=axis, keepdim=True, unbiased=False)
    return (x - m) / torch.sqrt(v + eps)
def _scatter_add(tgt, idx, src, axis=0):
    return tgt.clone().index_add_(axis, idx, src)
def _segment_sum(src, seg, n):
    out = torch.zeros((n,) + src.shape[1:], dtype=src.dtype)
    return out.index_add_(0, seg, src)
def _pad(x, pw):
    flat = tuple(v for pair in reversed(pw) for v in pair)
    return torch.nn.functional.pad(x, flat)

kernel_name = 'concat_heads__gqa_block_cached__mha_block_cached__decode'

def setup_inputs():

    _w = (lambda a: (lambda t: t.to(torch.bfloat16) if t.is_floating_point() else t)(torch.as_tensor(a)))
    def _A_setup():
        B, Sq, D = 2, 1, 576
        Hq, Hkv, Sk, Dh = 32, 16, 8192, 128
        assert Hq % Hkv == 0, (Hq, Hkv)
        return {
            "x": _w((lambda *s: torch.randn(*s, dtype=torch.bfloat16) * 0.8)(B, Sq, D)),
            "k_cache": _w((lambda *s: torch.randn(*s, dtype=torch.bfloat16) * 0.8)(B, Hkv, Sk, Dh)),
            "v_cache": _w((lambda *s: torch.randn(*s, dtype=torch.bfloat16) * 0.05)(B, Hkv, Sk, Dh)),
            "wq": _w((lambda *s: torch.randn(*s, dtype=torch.bfloat16) * 0.8)(D, Hq * Dh) * (D ** -0.5)),
            "wo": _w((lambda *s: torch.randn(*s, dtype=torch.bfloat16) * 0.05)(Hq * Dh, D) * ((Hq * Dh) ** -0.5)),
        }
    _A_in = _A_setup()

    def _B_setup():
        B, Sq, D = 2, 1, 576
        H, Sk, Dh = 32, 8192, 128
        return {
            "x": _w((lambda *s: torch.randn(*s, dtype=torch.bfloat16) * 0.8)(B, Sq, D)),
            "k_cache": _w((lambda *s: torch.randn(*s, dtype=torch.bfloat16) * 0.8)(B, H, Sk, Dh)),
            "v_cache": _w((lambda *s: torch.randn(*s, dtype=torch.bfloat16) * 0.05)(B, H, Sk, Dh)),
            "wq": _w((lambda *s: torch.randn(*s, dtype=torch.bfloat16) * 0.8)(D, H * Dh) * (D ** -0.5)),
            "wo": _w((lambda *s: torch.randn(*s, dtype=torch.bfloat16) * 0.05)(H * Dh, D) * ((H * Dh) ** -0.5)),
        }
    _B_in = _B_setup()

    return {
        "A_x": _A_in["x"],
        "A_k_cache": _A_in["k_cache"],
        "A_v_cache": _A_in["v_cache"],
        "A_wq": _A_in["wq"],
        "A_wo": _A_in["wo"],
        "B_x": _B_in["x"],
        "B_k_cache": _B_in["k_cache"],
        "B_v_cache": _B_in["v_cache"],
        "B_wq": _B_in["wq"],
        "B_wo": _B_in["wo"],
    }

def reference(A_x, A_k_cache, A_v_cache, A_wq, A_wo, B_x, B_k_cache, B_v_cache, B_wq, B_wo):
    def _A_ref(x, k_cache, v_cache, wq, wo):
        B, Sq, D = x.shape
        Hq, Dh = 32, 128
        Hkv = k_cache.shape[1]
        rep = Hq // Hkv
        q = (lambda x, *d: x.permute(*d))((x @ wq).reshape(B, Sq, Hq, Dh), 0, 2, 1, 3)
        k = (lambda x, n, axis: torch.repeat_interleave(x, n, dim=axis))(k_cache, rep, axis=1)
        v = (lambda x, n, axis: torch.repeat_interleave(x, n, dim=axis))(v_cache, rep, axis=1)
        scores = torch.einsum("bhqd,bhkd->bhqk", q, k) * (Dh ** -0.5)
        p = _softmax(scores, axis=-1)
        o = torch.einsum("bhqk,bhkd->bhqd", p, v)
        return (lambda x, *d: x.permute(*d))(o, 0, 2, 1, 3).reshape(B, Sq, Hq * Dh) @ wo

    def _B_ref(x, k_cache, v_cache, wq, wo):
        B, Sq, D = x.shape
        H, Dh = 32, 128
        q = (lambda x, *d: x.permute(*d))((x @ wq).reshape(B, Sq, H, Dh), 0, 2, 1, 3)
        scores = torch.einsum("bhqd,bhkd->bhqk", q, k_cache) * (Dh ** -0.5)
        p = _softmax(scores, axis=-1)
        o = torch.einsum("bhqk,bhkd->bhqd", p, v_cache)
        return (lambda x, *d: x.permute(*d))(o, 0, 2, 1, 3).reshape(B, Sq, H * Dh) @ wo

    a = _A_ref(A_x, A_k_cache, A_v_cache, A_wq, A_wo)
    b = _B_ref(B_x, B_k_cache, B_v_cache, B_wq, B_wo)
    return torch.concatenate([a, b], axis=-1)
```

```cpp
#include <hip/hip_runtime.h>
#include <hip/hip_bf16.h>
#include <cstdint>

using bf16x2 = __attribute__((ext_vector_type(2))) __bf16;
using u32x4  = __attribute__((ext_vector_type(4))) unsigned;
using f32x4  = __attribute__((ext_vector_type(4))) float;
typedef unsigned short u16;
typedef unsigned long long u64;
typedef __attribute__((address_space(1))) u64 gu64;
typedef __attribute__((address_space(1))) unsigned gu32;

#define DEPTH 2
#define BAR0 1
#define XSWZ 0
constexpr int DM = 576, DH = 128, SK = 8192, NQ = 4096;
constexpr int NT = 16;
constexpr float C2 = 0.088388347648318440f * 1.4426950408889634f;
constexpr unsigned SPIN_CAP = 1u << 22;

constexpr size_t WS_EPOCH = 0;
constexpr size_t WS_QG = 4096;
constexpr size_t WS_PG = WS_QG + 128 * 64 * 8;
constexpr size_t WS_YG = WS_PG + 128 * 8 * 136 * 8;

__device__ __forceinline__ float dot2(unsigned k, unsigned q, float acc) {
  return __builtin_amdgcn_fdot2_f32_bf16(__builtin_bit_cast(bf16x2, k), __builtin_bit_cast(bf16x2, q), acc, false);
}
template <int CTRL> __device__ __forceinline__ float dpp(float x) {
  return __builtin_bit_cast(float, __builtin_amdgcn_mov_dpp(__builtin_bit_cast(int, x), CTRL, 0xf, 0xf, true));
}
constexpr int XOR1 = 0xB1, XOR2 = 0x4E, XOR7 = 0x141, XOR8 = 0x128, ROR4 = 0x124;
__device__ __forceinline__ float row16_sum(float x) {
  x += dpp<XOR1>(x); x += dpp<XOR2>(x); x += dpp<XOR7>(x); x += dpp<XOR8>(x); return x;
}
__device__ __forceinline__ float xrow_sum(float x) {
  auto s = __builtin_amdgcn_permlane16_swap(__float_as_uint(x), __float_as_uint(x), false, false);
  x = __uint_as_float(s[0]) + __uint_as_float(s[1]);
  auto t = __builtin_amdgcn_permlane32_swap(__float_as_uint(x), __float_as_uint(x), false, false);
  return __uint_as_float(t[0]) + __uint_as_float(t[1]);
}
__device__ __forceinline__ float xrow_max(float x) {
  auto s = __builtin_amdgcn_permlane16_swap(__float_as_uint(x), __float_as_uint(x), false, false);
  x = fmaxf(__uint_as_float(s[0]), __uint_as_float(s[1]));
  auto t = __builtin_amdgcn_permlane32_swap(__float_as_uint(x), __float_as_uint(x), false, false);
  return fmaxf(__uint_as_float(t[0]), __uint_as_float(t[1]));
}
__device__ __forceinline__ float bflo(unsigned v) { return __builtin_bit_cast(float, v << 16); }
__device__ __forceinline__ float bfhi(unsigned v) { return __builtin_bit_cast(float, v & 0xffff0000u); }
__device__ __forceinline__ u16 f2bf(float f) { return __builtin_bit_cast(u16, (__bf16)f); }
__device__ __forceinline__ void st_gran(gu64* g, unsigned tag, unsigned val) {
  __hip_atomic_store(g, ((u64)tag << 32) | val, __ATOMIC_RELAXED, __HIP_MEMORY_SCOPE_AGENT);
}
__device__ __forceinline__ u64 ld_gran(gu64* g) { return __hip_atomic_load(g, __ATOMIC_RELAXED, __HIP_MEMORY_SCOPE_AGENT); }
__device__ __forceinline__ void bar() { asm volatile("s_waitcnt lgkmcnt(0)\n\ts_barrier" ::: "memory"); }

struct Params { const u16 *xA, *kA, *vA, *wqA, *woA, *xB, *kB, *vB, *wqB, *woB; u16* out; unsigned char* ws; };

constexpr int L_QP = 0;
constexpr int L_Q  = 256;
constexpr int L_TAG = 512;
constexpr int L_REC = 520;
constexpr int L_P2 = L_REC + 12 * 2 * 132;
constexpr int L_O  = L_P2 + 2 * 8 * 136;
constexpr int L_Y  = L_O + 256;
constexpr int L_Z  = L_Y + 4 * 2 * 192;
constexpr int L_END = L_Z + 768;

template <int G>
__device__ __forceinline__ void stream_wave(const u32x4* kb, const u32x4* vb, const u32x4* wop, bool has_wo, float* lds, int qoff, int sw, int lane, u32x4 (&W)[4]) {
  const int r = lane >> 4, c = lane & 15;
  u32x4 K[DEPTH][4], V[DEPTH][4];
  constexpr int TS = 32 * 256;
  bar();
#pragma unroll
  for (int d = 0; d < DEPTH; ++d) {
#pragma unroll
    for (int t = 0; t < 4; ++t) K[d][t] = __builtin_nontemporal_load(kb + d * TS + t * 64);
#pragma unroll
    for (int t = 0; t < 4; ++t) V[d][t] = __builtin_nontemporal_load(vb + d * TS + t * 64);
  }
  __builtin_amdgcn_sched_barrier(0);
  bar();
  bar();
  u32x4 q[G];
#pragma unroll
  for (int g = 0; g < G; ++g) q[g] = *reinterpret_cast<const u32x4*>(lds + L_Q + qoff + g * 64 + 4 * c);
  float m[G], l[G], acc[G][8];
#pragma unroll
  for (int g = 0; g < G; ++g) { m[g] = -INFINITY; l[g] = 0.f;
#pragma unroll
    for (int k = 0; k < 8; ++k) acc[g][k] = 0.f; }
#pragma unroll
  for (int i = 0; i < NT; ++i) {
    const int d = i % DEPTH;
    float s[G][4];
#pragma unroll
    for (int t = 0; t < 4; ++t)
#pragma unroll
      for (int g = 0; g < G; ++g) { float x = 0.f;
#pragma unroll
        for (int k = 0; k < 4; ++k) x = dot2(K[d][t][k], q[g][k], x);
        s[g][t] = x; }
    __builtin_amdgcn_sched_barrier(0);
    if (i + DEPTH < NT) {
#pragma unroll
      for (int t = 0; t < 4; ++t) K[d][t] = __builtin_nontemporal_load(kb + (i + DEPTH) * TS + t * 64);
    } else if (i + DEPTH == NT) {
      if (has_wo) {
#pragma unroll
        for (int t = 0; t < 4; ++t) W[t] = __builtin_nontemporal_load(wop + t * 8 * (DM / 8));
      }
    }
    __builtin_amdgcn_sched_barrier(0);
    float pr[G][4];
#pragma unroll
    for (int g = 0; g < G; ++g) {
#pragma unroll
      for (int t = 0; t < 4; ++t) s[g][t] = row16_sum(s[g][t]) * C2;
      const float mn = fmaxf(fmaxf(fmaxf(s[g][0], s[g][1]), fmaxf(s[g][2], s[g][3])), m[g]);
      const float al = __builtin_amdgcn_exp2f(m[g] - mn);
      m[g] = mn;
#pragma unroll
      for (int t = 0; t < 4; ++t) pr[g][t] = __builtin_amdgcn_exp2f(s[g][t] - mn);
      l[g] = fmaf(l[g], al, (pr[g][0] + pr[g][1]) + (pr[g][2] + pr[g][3]));
#pragma unroll
      for (int k = 0; k < 8; ++k) acc[g][k] *= al;
    }
#pragma unroll
    for (int t = 0; t < 4; ++t) {
#pragma unroll
      for (int k = 0; k < 4; ++k) {
        const float lo = bflo(V[d][t][k]), hi = bfhi(V[d][t][k]);
#pragma unroll
        for (int g = 0; g < G; ++g) { acc[g][2 * k] = fmaf(pr[g][t], lo, acc[g][2 * k]); acc[g][2 * k + 1] = fmaf(pr[g][t], hi, acc[g][2 * k + 1]); }
      }
    }
#pragma unroll
    for (int g = 0; g < G; ++g)
      asm volatile("" : "+v"(acc[g][0]), "+v"(acc[g][1]), "+v"(acc[g][2]), "+v"(acc[g][3]), "+v"(acc[g][4]), "+v"(acc[g][5]), "+v"(acc[g][6]), "+v"(acc[g][7]), "+v"(l[g]));
    __builtin_amdgcn_sched_barrier(0);
    if (i + DEPTH < NT) {
#pragma unroll
      for (int t = 0; t < 4; ++t) V[d][t] = __builtin_nontemporal_load(vb + (i + DEPTH) * TS + t * 64);
    }
    __builtin_amdgcn_sched_barrier(0);
  }
#pragma unroll
  for (int g = 0; g < G; ++g) {
    const float M = xrow_max(m[g]);
    const float w = __builtin_amdgcn_exp2f(m[g] - M);
    l[g] = xrow_sum(l[g] * w);
#pragma unroll
    for (int k = 0; k < 8; ++k) acc[g][k] = xrow_sum(acc[g][k] * w);
    m[g] = M;
  }
  if (r == 0) {
#pragma unroll
    for (int g = 0; g < G; ++g) {
      float* rec = lds + L_REC + (sw * 2 + g) * 132;
      *reinterpret_cast<f32x4*>(rec + 8 * c) = f32x4{acc[g][0], acc[g][1], acc[g][2], acc[g][3]};
      *reinterpret_cast<f32x4*>(rec + 4 + 8 * c) = f32x4{acc[g][4], acc[g][5], acc[g][6], acc[g][7]};
      if (c == 0) { rec[128] = m[g]; rec[129] = l[g]; }
    }
  }
}

__device__ __forceinline__ int stream_row(int st) { return st < 32 ? (st >> 4) * 32 + (st & 15) * 2 : 64 + (st - 32); }

__global__ void __launch_bounds__(1024) k_fused(Params p) {
  __shared__ __attribute__((aligned(16))) float lds[L_END];
  const int tid = threadIdx.x, lane = tid & 63, wave = __builtin_amdgcn_readfirstlane(tid >> 6);
  const int bx = blockIdx.x;
  const int wg = XSWZ ? (bx & 7) * 32 + (bx >> 3) : bx;
  const int st0 = (wg * 12) >> 5;
  gu64* QG = (gu64*)(p.ws + WS_QG); gu64* PG = (gu64*)(p.ws + WS_PG); gu64* YG = (gu64*)(p.ws + WS_YG);
  const bool has2 = bx < 192;
  const int m2 = bx / 96, h2 = (bx % 96) / 3, cg2 = bx % 3;
  u32x4 W[4];
  if (wave < 4) {
    __builtin_amdgcn_s_setprio(3);
    const int cs = (bx & ~15) | ((bx & 7) << 1) | ((bx >> 3) & 1);
    const int m = cs >> 7, col0 = (cs & 127) * 32;
    const u16* x = m ? p.xB : p.xA; const u16* wq = m ? p.wqB : p.wqA;
    const int ch = lane & 3, r16 = lane >> 2, row0 = wave * 144 + r16;
    u32x4 w[9]; u16 xv[2][9];
#pragma unroll
    for (int i = 0; i < 9; ++i) w[i] = __builtin_nontemporal_load(reinterpret_cast<const u32x4*>(wq + (size_t)(row0 + 16 * i) * NQ + col0 + ch * 8));
    const unsigned e0 = __hip_atomic_load((gu32*)(p.ws + WS_EPOCH), __ATOMIC_RELAXED, __HIP_MEMORY_SCOPE_AGENT);
#pragma unroll
    for (int i = 0; i < 9; ++i) { xv[0][i] = x[row0 + 16 * i]; xv[1][i] = x[DM + row0 + 16 * i]; }
    __builtin_amdgcn_sched_barrier(0);
    bar();
    __builtin_amdgcn_sched_barrier(0);
    const unsigned tag = e0 * 1664525u + 1013904223u;
    float a[2][8];
#pragma unroll
    for (int b = 0; b < 2; ++b)
#pragma unroll
      for (int k = 0; k < 8; ++k) a[b][k] = 0.f;
#pragma unroll
    for (int i = 0; i < 9; ++i) {
      const float x0 = bflo(xv[0][i]), x1 = bflo(xv[1][i]);
#pragma unroll
      for (int k = 0; k < 4; ++k) {
        const float lo = bflo(w[i][k]), hi = bfhi(w[i][k]);
        a[0][2 * k] = fmaf(x0, lo, a[0][2 * k]); a[0][2 * k + 1] = fmaf(x0, hi, a[0][2 * k + 1]);
        a[1][2 * k] = fmaf(x1, lo, a[1][2 * k]); a[1][2 * k + 1] = fmaf(x1, hi, a[1][2 * k + 1]);
      }
    }
#pragma unroll
    for (int b = 0; b < 2; ++b)
#pragma unroll
      for (int k = 0; k < 8; ++k) { float v = a[b][k]; v += dpp<ROR4>(v); v += dpp<XOR8>(v); a[b][k] = xrow_sum(v); }
    if (lane < 4) {
#pragma unroll
      for (int b = 0; b < 2; ++b) {
        *reinterpret_cast<f32x4*>(lds + L_QP + (wave * 2 + b) * 32 + lane * 8) = f32x4{a[b][0], a[b][1], a[b][2], a[b][3]};
        *reinterpret_cast<f32x4*>(lds + L_QP + (wave * 2 + b) * 32 + lane * 8 + 4) = f32x4{a[b][4], a[b][5], a[b][6], a[b][7]};
      }
    }
    if (tid == 0) reinterpret_cast<unsigned*>(lds)[L_TAG] = tag;
    bar();
    if (wave == 0 && lane < 32) {
      const int b = lane >> 4, cp = lane & 15;
      float v0 = 0.f, v1 = 0.f;
#pragma unroll
      for (int w4 = 0; w4 < 4; ++w4) { v0 += lds[L_QP + (w4 * 2 + b) * 32 + 2 * cp]; v1 += lds[L_QP + (w4 * 2 + b) * 32 + 2 * cp + 1]; }
      const int row = (m * 2 + b) * 32 + (col0 >> 7);
      st_gran(QG + row * 64 + ((col0 & 127) >> 1) + cp, tag, (unsigned)f2bf(v0) | ((unsigned)f2bf(v1) << 16));
    }
    {
      const int seg = wave >> 1, g = wave & 1, st = st0 + seg;
      const bool need = (st * 32 < wg * 12 + 12) && (g == 0 || st < 32);
      if (need) {
        gu64* src = QG + (stream_row(st) + g) * 64 + lane;
        u64 v;
        for (unsigned spin = 0; spin < SPIN_CAP; ++spin) {
          v = ld_gran(src);
          if (__all((unsigned)(v >> 32) == tag)) break;
          __builtin_amdgcn_s_sleep(2);
        }
        reinterpret_cast<unsigned*>(lds)[L_Q + wave * 64 + lane] = (unsigned)v;
      }
    }
    bar();
    __builtin_amdgcn_s_setprio(0);
  } else {
    const int sw = wave - 4, gw = wg * 12 + sw, st = gw >> 5, j = gw & 31;
    const size_t off = ((size_t)(st < 32 ? st : st - 32) * SK * DH) / 8 + (size_t)j * 256 + lane;
    const u16* wo = m2 ? p.woB : p.woA;
    const u32x4* wop = reinterpret_cast<const u32x4*>(wo + (size_t)(h2 * 128 + (sw / 3) * 32 + (lane >> 3)) * DM + cg2 * 192 + (sw % 3) * 64 + (lane & 7) * 8);
    const int qoff = (st - st0) * 128;
    if (st < 32) stream_wave<2>(reinterpret_cast<const u32x4*>(p.kA) + off, reinterpret_cast<const u32x4*>(p.vA) + off, wop, has2, lds, qoff, sw, lane, W);
    else         stream_wave<1>(reinterpret_cast<const u32x4*>(p.kB) + off, reinterpret_cast<const u32x4*>(p.vB) + off, wop, has2, lds, qoff, sw, lane, W);
  }
  bar();
  const unsigned tag = reinterpret_cast<const unsigned*>(lds)[L_TAG];
  {
    const int u = tid / 260, rem = tid % 260, g = rem / 130, e = rem % 130;
    const int gu = wg * 3 + u, st = gu >> 3, sp = gu & 7;
    if (u < 3 && (g == 0 || st < 32)) {
      const float* rec = lds + L_REC + (u * 8 + g) * 132;
      const float m0 = rec[128], m1 = rec[264 + 128], m2_ = rec[528 + 128], m3 = rec[792 + 128];
      const float M = fmaxf(fmaxf(m0, m1), fmaxf(m2_, m3));
      const int ee = e == 128 ? 129 : e;
      float v = __builtin_amdgcn_exp2f(m0 - M) * rec[ee];
      v = fmaf(__builtin_amdgcn_exp2f(m1 - M), rec[264 + ee], v);
      v = fmaf(__builtin_amdgcn_exp2f(m2_ - M), rec[528 + ee], v);
      v = fmaf(__builtin_amdgcn_exp2f(m3 - M), rec[792 + ee], v);
      if (e == 128) v = M;
      st_gran(PG + ((stream_row(st) + g) * 8 + sp) * 136 + e, tag, __float_as_uint(v));
    }
  }
  if (has2) {
    unsigned pend = 0; int lo_[3]; gu64* src[3];
#pragma unroll
    for (int k = 0; k < 3; ++k) {
      const int idx = tid + 1024 * k, bs = idx / 130, e = idx % 130, b = bs >> 3, s = bs & 7;
      if (idx < 2080) pend |= 1u << k;
      src[k] = PG + ((((m2 * 2 + (b & 1)) * 32 + h2) * 8 + s) * 136 + e);
      lo_[k] = L_P2 + bs * 136 + e;
    }
    for (unsigned spin = 0; spin < SPIN_CAP; ++spin) {
      u64 v[3];
#pragma unroll
      for (int k = 0; k < 3; ++k) if (pend & (1u << k)) v[k] = ld_gran(src[k]);
#pragma unroll
      for (int k = 0; k < 3; ++k) if ((pend & (1u << k)) && (unsigned)(v[k] >> 32) == tag) { pend &= ~(1u << k); lds[lo_[k]] = __uint_as_float((unsigned)v[k]); }
      if (__all(pend == 0)) break;
      __builtin_amdgcn_s_sleep(4);
    }
    bar();
    if (tid < 256) {
      const int b = tid >> 7, dd = tid & 127;
      const float* base = lds + L_P2 + b * 8 * 136;
      float M = base[128];
#pragma unroll
      for (int s = 1; s < 8; ++s) M = fmaxf(M, base[s * 136 + 128]);
      float L = 0.f, o = 0.f;
#pragma unroll
      for (int s = 0; s < 8; ++s) { const float wt = __builtin_amdgcn_exp2f(base[s * 136 + 128] - M); L = fmaf(wt, base[s * 136 + 129], L); o = fmaf(wt, base[s * 136 + dd], o); }
      lds[L_O + tid] = o / L;
    }
    bar();
    if (wave >= 4) {
      const int sw = wave - 4, rb = sw / 3, cs = sw % 3, r8 = lane >> 3, ch = lane & 7;
      float a[2][8];
#pragma unroll
      for (int b = 0; b < 2; ++b)
#pragma unroll
        for (int k = 0; k < 8; ++k) a[b][k] = 0.f;
#pragma unroll
      for (int t = 0; t < 4; ++t) {
        const float x0 = lds[L_O + rb * 32 + r8 + 8 * t], x1 = lds[L_O + 128 + rb * 32 + r8 + 8 * t];
#pragma unroll
        for (int k = 0; k < 4; ++k) {
          const float lo = bflo(W[t][k]), hi = bfhi(W[t][k]);
          a[0][2 * k] = fmaf(x0, lo, a[0][2 * k]); a[0][2 * k + 1] = fmaf(x0, hi, a[0][2 * k + 1]);
          a[1][2 * k] = fmaf(x1, lo, a[1][2 * k]); a[1][2 * k + 1] = fmaf(x1, hi, a[1][2 * k + 1]);
        }
      }
#pragma unroll
      for (int b = 0; b < 2; ++b)
#pragma unroll
        for (int k = 0; k < 8; ++k) { float v = a[b][k]; v += dpp<XOR8>(v); a[b][k] = xrow_sum(v); }
      if (lane < 8) {
#pragma unroll
        for (int b = 0; b < 2; ++b) {
          float* y = lds + L_Y + (rb * 2 + b) * 192 + cs * 64 + ch * 8;
          *reinterpret_cast<f32x4*>(y) = f32x4{a[b][0], a[b][1], a[b][2], a[b][3]};
          *reinterpret_cast<f32x4*>(y + 4) = f32x4{a[b][4], a[b][5], a[b][6], a[b][7]};
        }
      }
    }
    bar();
    if (tid < 384) {
      const int b = tid / 192, cc = tid % 192;
      const float v = (lds[L_Y + b * 192 + cc] + lds[L_Y + (2 + b) * 192 + cc]) + (lds[L_Y + (4 + b) * 192 + cc] + lds[L_Y + (6 + b) * 192 + cc]);
      st_gran(YG + ((m2 * 2 + b) * 32 + h2) * DM + cg2 * 192 + cc, tag, __float_as_uint(v));
    }
  }
  if (bx >= 160) {
    const int o3 = bx - 160, mb = o3 / 24, c0 = (o3 % 24) * 24;
    if (tid < 768) {
      const int hh = tid / 24, cc = tid % 24;
      gu64* src = YG + (mb * 32 + hh) * DM + c0 + cc;
      bool pend = true;
      for (unsigned spin = 0; spin < SPIN_CAP; ++spin) {
        if (pend) { const u64 v = ld_gran(src); if ((unsigned)(v >> 32) == tag) { pend = false; lds[L_Z + tid] = __uint_as_float((unsigned)v); } }
        if (__all(!pend)) break;
        __builtin_amdgcn_s_sleep(4);
      }
    } else if (bx == 160 && wave == 12) {
      gu64* src = YG + (64 + lane) * DM;
      for (unsigned spin = 0; spin < SPIN_CAP; ++spin) {
        const u64 v = ld_gran(src);
        if (__all((unsigned)(v >> 32) == tag)) break;
        __builtin_amdgcn_s_sleep(4);
      }
    }
    bar();
    if (tid < 24) {
      float v = 0.f;
#pragma unroll
      for (int hh = 0; hh < 32; ++hh) v += lds[L_Z + hh * 24 + tid];
      p.out[(mb & 1) * 2 * DM + (mb >> 1) * DM + c0 + tid] = f2bf(v);
    }
    if (bx == 160 && tid == 64) __hip_atomic_store((gu32*)(p.ws + WS_EPOCH), tag, __ATOMIC_RELAXED, __HIP_MEMORY_SCOPE_AGENT);
  }
}

extern "C" void kernel_launch(void* const* d_in, const int* in_sizes, int n_in, void* d_out, int out_size, void* d_ws, size_t ws_size, hipStream_t stream) {
  Params p;
  p.xA = (const u16*)d_in[0]; p.kA = (const u16*)d_in[1]; p.vA = (const u16*)d_in[2]; p.wqA = (const u16*)d_in[3]; p.woA = (const u16*)d_in[4];
  p.xB = (const u16*)d_in[5]; p.kB = (const u16*)d_in[6]; p.vB = (const u16*)d_in[7]; p.wqB = (const u16*)d_in[8]; p.woB = (const u16*)d_in[9];
  p.out = (u16*)d_out; p.ws = (unsigned char*)d_ws;
  k_fused<<<256, 1024, 0, stream>>>(p);
}
```

```cpp
#include <hip/hip_runtime.h>
#include <hip/hip_bf16.h>
#include <cstdint>
#include <type_traits>

using bf16x2 = __attribute__((ext_vector_type(2))) __bf16;
using u32x4  = __attribute__((ext_vector_type(4))) unsigned;
using f32x4  = __attribute__((ext_vector_type(4))) float;
using bf16x8 = __attribute__((ext_vector_type(8))) __bf16;
typedef unsigned short u16;
typedef unsigned long long u64;
typedef __attribute__((address_space(1))) u64 gu64;
typedef __attribute__((address_space(1))) unsigned gu32;

#define QSS "4"
constexpr int DM = 576, DH = 128, SK = 8192, NQ = 4096;
constexpr int NT = 16;
constexpr float C2 = 0.088388347648318440f * 1.4426950408889634f;
constexpr unsigned SPIN_CAP = 1u << 16;

constexpr size_t WS_EPOCH = 0;
constexpr size_t WS_QG = 4096;
constexpr size_t WS_PG = WS_QG + 128 * 64 * 8;
constexpr size_t WS_YG = WS_PG + 128 * 8 * 136 * 8;

__device__ __forceinline__ float dot2(unsigned k, unsigned q, float acc) {
  return __builtin_amdgcn_fdot2_f32_bf16(__builtin_bit_cast(bf16x2, k), __builtin_bit_cast(bf16x2, q), acc, false);
}
template <int CTRL> __device__ __forceinline__ float dpp(float x) {
  return __builtin_bit_cast(float, __builtin_amdgcn_mov_dpp(__builtin_bit_cast(int, x), CTRL, 0xf, 0xf, true));
}
constexpr int XOR1 = 0xB1, XOR2 = 0x4E, XOR7 = 0x141, XOR8 = 0x128, ROR4 = 0x124;
__device__ __forceinline__ float row16_sum(float x) {
  x += dpp<XOR1>(x); x += dpp<XOR2>(x); x += dpp<XOR7>(x); x += dpp<XOR8>(x); return x;
}
__device__ __forceinline__ float xrow_sum(float x) {
  auto s = __builtin_amdgcn_permlane16_swap(__float_as_uint(x), __float_as_uint(x), false, false);
  x = __uint_as_float(s[0]) + __uint_as_float(s[1]);
  auto t = __builtin_amdgcn_permlane32_swap(__float_as_uint(x), __float_as_uint(x), false, false);
  return __uint_as_float(t[0]) + __uint_as_float(t[1]);
}
__device__ __forceinline__ float xrow_max(float x) {
  auto s = __builtin_amdgcn_permlane16_swap(__float_as_uint(x), __float_as_uint(x), false, false);
  x = fmaxf(__uint_as_float(s[0]), __uint_as_float(s[1]));
  auto t = __builtin_amdgcn_permlane32_swap(__float_as_uint(x), __float_as_uint(x), false, false);
  return fmaxf(__uint_as_float(t[0]), __uint_as_float(t[1]));
}
__device__ __forceinline__ float bflo(unsigned v) { return __builtin_bit_cast(float, v << 16); }
__device__ __forceinline__ float bfhi(unsigned v) { return __builtin_bit_cast(float, v & 0xffff0000u); }
__device__ __forceinline__ u16 f2bf(float f) { return __builtin_bit_cast(u16, (__bf16)f); }
__device__ __forceinline__ void st_gran(gu64* g, unsigned tag, unsigned val) {
  __hip_atomic_store(g, ((u64)tag << 32) | val, __ATOMIC_RELAXED, __HIP_MEMORY_SCOPE_AGENT);
}
__device__ __forceinline__ u64 ld_gran(gu64* g) { return __hip_atomic_load(g, __ATOMIC_RELAXED, __HIP_MEMORY_SCOPE_AGENT); }
__device__ __forceinline__ void bar() { asm volatile("s_waitcnt lgkmcnt(0)\n\ts_barrier" ::: "memory"); }


struct Params { const u16 *xA, *kA, *vA, *wqA, *woA, *xB, *kB, *vB, *wqB, *woB; u16* out; unsigned char* ws; };

constexpr int L_QP = 0;
constexpr int L_Q  = 256;
constexpr int L_TAG = 512, L_CNT = 513;
constexpr int L_O  = 520;
constexpr int L_REC = 648;
constexpr int L_W  = L_REC + 16 * 132;
constexpr int L_END = L_W + 4 * 128 * 40;

template <int G>
__device__ __forceinline__ void stream_wave(const char* kb, const char* vb, int j, float* lds, int qoff, int rec0, int lane) {
  const int r = lane >> 4, c = lane & 15;
  const unsigned lo16 = lane * 16;
  u32x4 K[2][4], V[2][4];
  constexpr int AK = 2, AV = 2;
  const __amdgpu_buffer_rsrc_t kr = __builtin_amdgcn_make_buffer_rsrc((void*)kb, 0, SK * DH * 2, 0x00020000);
  const __amdgpu_buffer_rsrc_t vr = __builtin_amdgcn_make_buffer_rsrc((void*)vb, 0, SK * DH * 2, 0x00020000);
  bar();
#pragma unroll
  for (int d = 0; d < 2; ++d) {
#pragma unroll
    for (int t = 0; t < 4; ++t) K[d][t] = __builtin_amdgcn_raw_buffer_load_b128(kr, lo16 + t * 1024, (d * 32 + j) * 4096, AK);
#pragma unroll
    for (int t = 0; t < 4; ++t) V[d][t] = __builtin_amdgcn_raw_buffer_load_b128(vr, lo16 + t * 1024, (d * 32 + j) * 4096, AV);
  }
  __builtin_amdgcn_sched_barrier(0);
  bar();
  u32x4 q[G];
#pragma unroll
  for (int g = 0; g < G; ++g) q[g] = *reinterpret_cast<const u32x4*>(lds + L_Q + qoff + g * 64 + 4 * c);
  float m[G], l[G], acc[G][8];
#pragma unroll
  for (int g = 0; g < G; ++g) { m[g] = -INFINITY; l[g] = 0.f;
#pragma unroll
    for (int k = 0; k < 8; ++k) acc[g][k] = 0.f; }
  auto step = [&](auto dc, int ix) __attribute__((always_inline)) {
      constexpr int d = decltype(dc)::value;
      float s[G][4];
#pragma unroll
      for (int t = 0; t < 4; ++t)
#pragma unroll
        for (int g = 0; g < G; ++g) { float x = 0.f;
#pragma unroll
          for (int k = 0; k < 4; ++k) x = dot2(K[d][t][k], q[g][k], x);
          s[g][t] = x; }
      const bool valid = ix < NT;
      const int toff = valid ? (ix * 32 + j) * 4096 : 0;
      const unsigned lof = valid ? lo16 : 0x40000000u;
      __builtin_amdgcn_sched_barrier(0);
#pragma unroll
      for (int t = 0; t < 4; ++t) K[d][t] = __builtin_amdgcn_raw_buffer_load_b128(kr, lof + t * 1024, toff, AK);
      __builtin_amdgcn_sched_barrier(0);
      float pr[G][4];
#pragma unroll
      for (int g = 0; g < G; ++g) {
#pragma unroll
        for (int t = 0; t < 4; ++t) s[g][t] = row16_sum(s[g][t]) * C2;
        const float mn = fmaxf(fmaxf(fmaxf(s[g][0], s[g][1]), fmaxf(s[g][2], s[g][3])), m[g]);
        const float al = __builtin_amdgcn_exp2f(m[g] - mn);
        m[g] = mn;
#pragma unroll
        for (int t = 0; t < 4; ++t) pr[g][t] = __builtin_amdgcn_exp2f(s[g][t] - mn);
        l[g] = fmaf(l[g], al, (pr[g][0] + pr[g][1]) + (pr[g][2] + pr[g][3]));
#pragma unroll
        for (int k = 0; k < 8; ++k) acc[g][k] *= al;
      }
#pragma unroll
      for (int t = 0; t < 4; ++t) {
#pragma unroll
        for (int k = 0; k < 4; ++k) {
          const float lo = bflo(V[d][t][k]), hi = bfhi(V[d][t][k]);
#pragma unroll
          for (int g = 0; g < G; ++g) { acc[g][2 * k] = fmaf(pr[g][t], lo, acc[g][2 * k]); acc[g][2 * k + 1] = fmaf(pr[g][t], hi, acc[g][2 * k + 1]); }
        }
      }
#pragma unroll
      for (int g = 0; g < G; ++g)
        asm volatile("" : "+v"(acc[g][0]), "+v"(acc[g][1]), "+v"(acc[g][2]), "+v"(acc[g][3]), "+v"(acc[g][4]), "+v"(acc[g][5]), "+v"(acc[g][6]), "+v"(acc[g][7]), "+v"(l[g]));
      __builtin_amdgcn_sched_barrier(0);
#pragma unroll
      for (int t = 0; t < 4; ++t) V[d][t] = __builtin_amdgcn_raw_buffer_load_b128(vr, lof + t * 1024, toff, AV);
      __builtin_amdgcn_sched_barrier(0);
  };
#pragma nounroll
  for (int i = 0; i < NT; i += 2) {
    step(std::integral_constant<int, 0>{}, i + 2);
    step(std::integral_constant<int, 1>{}, i + 3);
  }
#pragma unroll
  for (int g = 0; g < G; ++g) {
    const float M = xrow_max(m[g]);
    const float w = __builtin_amdgcn_exp2f(m[g] - M);
    l[g] = xrow_sum(l[g] * w);
#pragma unroll
    for (int k = 0; k < 8; ++k) acc[g][k] = xrow_sum(acc[g][k] * w);
    m[g] = M;
  }
  if (r == 0) {
#pragma unroll
    for (int g = 0; g < G; ++g) {
      float* rec = lds + L_REC + (rec0 + g * 4) * 132;
      *reinterpret_cast<f32x4*>(rec + 8 * c) = f32x4{acc[g][0], acc[g][1], acc[g][2], acc[g][3]};
      *reinterpret_cast<f32x4*>(rec + 4 + 8 * c) = f32x4{acc[g][4], acc[g][5], acc[g][6], acc[g][7]};
      if (c == 0) { rec[128] = m[g]; rec[129] = l[g]; }
    }
  }
}

__global__ void __launch_bounds__(1024) k_fused(Params p) {
  __shared__ __attribute__((aligned(16))) float lds[L_END];
  unsigned* ldsu = reinterpret_cast<unsigned*>(lds);
  const int tid = threadIdx.x, lane = tid & 63, wave = __builtin_amdgcn_readfirstlane(tid >> 6);
  const int bx = blockIdx.x;
  const int grp = ((bx & 7) << 2) | (bx >> 6);
  const int sp = (bx >> 3) & 7;
  gu64* QG = (gu64*)(p.ws + WS_QG); gu64* PG = (gu64*)(p.ws + WS_PG); gu64* YG = (gu64*)(p.ws + WS_YG);
  const int rowA = (grp >> 4) * 32 + (grp & 15) * 2, rowB = 64 + 2 * grp;
  if (wave >= 4) {
    const int sw = wave - 4, u = sw >> 2, j = sp * 4 + (sw & 3);
    if (u == 0) stream_wave<2>((const char*)p.kA + (size_t)grp * SK * DH * 2, (const char*)p.vA + (size_t)grp * SK * DH * 2, j, lds, 0, sw, lane);
    else { const int st = 2 * grp + u - 1;
           stream_wave<1>((const char*)p.kB + (size_t)st * SK * DH * 2, (const char*)p.vB + (size_t)st * SK * DH * 2, j, lds, (u + 1) * 64, sw + 4, lane); }
    bar();
    const int t2 = tid - 256, v = t2 / 130, e = t2 % 130;
    if (v < 4) {
      const unsigned tag = ldsu[L_TAG];
      const float* rec = lds + L_REC + v * 4 * 132;
      const float m0 = rec[128], m1 = rec[132 + 128], m2 = rec[264 + 128], m3 = rec[396 + 128];
      const float M = fmaxf(fmaxf(m0, m1), fmaxf(m2, m3));
      float x = __builtin_amdgcn_exp2f(m0 - M) * rec[e];
      x = fmaf(__builtin_amdgcn_exp2f(m1 - M), rec[132 + e], x);
      x = fmaf(__builtin_amdgcn_exp2f(m2 - M), rec[264 + e], x);
      x = fmaf(__builtin_amdgcn_exp2f(m3 - M), rec[396 + e], x);
      if (e == 128) x = M;
      const int row = v < 2 ? rowA + v : rowB + v - 2;
      st_gran(PG + (row * 8 + sp) * 136 + e, tag, __float_as_uint(x));
    }
    return;
  }
  __builtin_amdgcn_s_setprio(3);
  unsigned tag;
  {
    const int cs = (bx & ~15) | ((bx & 7) << 1) | ((bx >> 3) & 1);
    const int m = cs >> 7, col0 = (cs & 127) * 32, hq = col0 >> 7;
    const u16* x = m ? p.xB : p.xA; const u16* wq = m ? p.wqB : p.wqA;
    const int ch = lane & 3, r16 = lane >> 2, row0 = wave * 144 + r16;
    u32x4 w[9]; u16 xv[2][9];
#pragma unroll
    for (int i = 0; i < 9; ++i) w[i] = __builtin_nontemporal_load(reinterpret_cast<const u32x4*>(wq + (size_t)(row0 + 16 * i) * NQ + col0 + ch * 8));
    const unsigned e0 = __hip_atomic_load((gu32*)(p.ws + WS_EPOCH), __ATOMIC_RELAXED, __HIP_MEMORY_SCOPE_AGENT);
#pragma unroll
    for (int i = 0; i < 9; ++i) { xv[0][i] = x[row0 + 16 * i]; xv[1][i] = x[DM + row0 + 16 * i]; }
    if (tid == 0) ldsu[L_CNT] = 0u;
    __builtin_amdgcn_sched_barrier(0);
    bar();
    __builtin_amdgcn_sched_barrier(0);
    tag = __builtin_amdgcn_readfirstlane(e0) * 1664525u + 1013904223u;
    float a[2][8];
#pragma unroll
    for (int b = 0; b < 2; ++b)
#pragma unroll
      for (int k = 0; k < 8; ++k) a[b][k] = 0.f;
#pragma unroll
    for (int i = 0; i < 9; ++i) {
      const float x0 = bflo(xv[0][i]), x1 = bflo(xv[1][i]);
#pragma unroll
      for (int k = 0; k < 4; ++k) {
        const float lo = bflo(w[i][k]), hi = bfhi(w[i][k]);
        a[0][2 * k] = fmaf(x0, lo, a[0][2 * k]); a[0][2 * k + 1] = fmaf(x0, hi, a[0][2 * k + 1]);
        a[1][2 * k] = fmaf(x1, lo, a[1][2 * k]); a[1][2 * k + 1] = fmaf(x1, hi, a[1][2 * k + 1]);
      }
    }
#pragma unroll
    for (int b = 0; b < 2; ++b)
#pragma unroll
      for (int k = 0; k < 8; ++k) { float v = a[b][k]; v += dpp<ROR4>(v); v += dpp<XOR8>(v); a[b][k] = xrow_sum(v); }
    if (lane < 4) {
#pragma unroll
      for (int b = 0; b < 2; ++b) {
        *reinterpret_cast<f32x4*>(lds + L_QP + (wave * 2 + b) * 32 + lane * 8) = f32x4{a[b][0], a[b][1], a[b][2], a[b][3]};
        *reinterpret_cast<f32x4*>(lds + L_QP + (wave * 2 + b) * 32 + lane * 8 + 4) = f32x4{a[b][4], a[b][5], a[b][6], a[b][7]};
      }
    }
    if (tid == 0) ldsu[L_TAG] = tag;
    asm volatile("s_waitcnt lgkmcnt(0)" ::: "memory");
    unsigned old = 0;
    if (lane == 0) old = __hip_atomic_fetch_add(ldsu + L_CNT, 1u, __ATOMIC_RELAXED, __HIP_MEMORY_SCOPE_WORKGROUP);
    old = __builtin_amdgcn_readfirstlane(old);
    asm volatile("" ::: "memory");
    if (old == 3 && lane < 32) {
      const int b = lane >> 4, cp = lane & 15;
      float v0 = 0.f, v1 = 0.f;
#pragma unroll
      for (int w4 = 0; w4 < 4; ++w4) { v0 += lds[L_QP + (w4 * 2 + b) * 32 + 2 * cp]; v1 += lds[L_QP + (w4 * 2 + b) * 32 + 2 * cp + 1]; }
      st_gran(QG + ((m * 2 + b) * 32 + hq) * 64 + ((col0 & 127) >> 1) + cp, tag, (unsigned)f2bf(v0) | ((unsigned)f2bf(v1) << 16));
    }
    gu64* src = QG + (wave < 2 ? rowA + wave : rowB + wave - 2) * 64 + lane;
    u64 v, r0, r1, r2, r3, tmp; unsigned cnt;
    const u64 tag64 = tag;
#define PSTEP(R, L) "s_waitcnt vmcnt(3)\n\tv_lshrrev_b64 %[t], 32, " R "\n\tv_cmp_ne_u64 vcc, %[g], %[t]\n\tv_mov_b64 %[v], " R "\n\ts_cbranch_vccz 2f\n\tglobal_load_dwordx2 " R ", %[p], off sc1\n\t"
    asm volatile(
      "global_load_dwordx2 %[a], %[p], off sc1\n\ts_sleep " QSS "\n\tglobal_load_dwordx2 %[b], %[p], off sc1\n\ts_sleep " QSS "\n\t"
      "global_load_dwordx2 %[c], %[p], off sc1\n\ts_sleep " QSS "\n\tglobal_load_dwordx2 %[d], %[p], off sc1\n\ts_movk_i32 %[n], 0x4000\n"
      "1:\n\t" PSTEP("%[a]", 0) PSTEP("%[b]", 0) PSTEP("%[c]", 0) PSTEP("%[d]", 0)
      "s_sub_u32 %[n], %[n], 1\n\ts_cbranch_scc0 1b\n"
      "2:"
      : [a] "=&v"(r0), [b] "=&v"(r1), [c] "=&v"(r2), [d] "=&v"(r3), [t] "=&v"(tmp), [v] "=&v"(v), [n] "=&s"(cnt)
      : [p] "v"(src), [g] "v"(tag64) : "vcc", "scc", "memory");
    ldsu[L_Q + wave * 64 + lane] = (unsigned)v;
    bar();
    asm volatile("s_waitcnt vmcnt(0)" :: "v"(r0), "v"(r1), "v"(r2), "v"(r3) : "memory");
    __builtin_amdgcn_s_setprio(0);
  }
  if ((bx & 1) == 0) {
    const int job = bx >> 1, m2 = job >> 6, h2 = (job >> 1) & 31;
    const int nt = wave < 2 ? 5 : 4, c0 = (job & 1) * 288 + (wave < 2 ? wave * 80 : 32 + wave * 64);
    const u16* wo = (m2 ? p.woB : p.woA) + (size_t)h2 * 128 * DM + c0;
    u32x4 Bf[5][4];
    {
      unsigned* stg = ldsu + L_W + wave * 128 * 40;
      if (wave < 2) {
#pragma unroll
        for (int i = 0; i < 20; ++i) { const int idx = lane + 64 * i, row = idx / 10, ch = idx % 10;
          *reinterpret_cast<u32x4*>(stg + row * 40 + ch * 4) = __builtin_nontemporal_load(reinterpret_cast<const u32x4*>(wo + (size_t)row * DM + ch * 8)); }
      } else {
#pragma unroll
        for (int i = 0; i < 16; ++i) { const int idx = lane + 64 * i, row = idx >> 3, ch = idx & 7;
          *reinterpret_cast<u32x4*>(stg + row * 40 + ch * 4) = __builtin_nontemporal_load(reinterpret_cast<const u32x4*>(wo + (size_t)row * DM + ch * 8)); }
      }
      asm volatile("s_waitcnt lgkmcnt(0)" ::: "memory");
      const u16* s16 = reinterpret_cast<const u16*>(stg);
#pragma unroll
      for (int ct = 0; ct < 5; ++ct)
#pragma unroll
        for (int ks = 0; ks < 4; ++ks)
#pragma unroll
          for (int e = 0; e < 4; ++e) { const int k0 = ks * 32 + (lane >> 4) * 8 + 2 * e, col = (ct < nt ? ct : 0) * 16 + (lane & 15);
            Bf[ct][ks][e] = (unsigned)s16[k0 * 80 + col] | ((unsigned)s16[(k0 + 1) * 80 + col] << 16); }
#pragma unroll
      for (int ct = 0; ct < 5; ++ct) asm volatile("" : "+v"(Bf[ct][0]), "+v"(Bf[ct][1]), "+v"(Bf[ct][2]), "+v"(Bf[ct][3]));
    }
    const int b = wave >> 1, dd = (wave & 1) * 64 + lane;
    gu64* src = PG + (size_t)((m2 * 2 + b) * 32 + h2) * 8 * 136;
    gu64* dst = YG + (size_t)(m2 * 64 + h2) * DM + c0 + (lane & 15);
    bar();
    u64 a[8], ml;
    for (unsigned spin = 0; spin < SPIN_CAP; ++spin) {
#pragma unroll
      for (int s = 0; s < 8; ++s) a[s] = ld_gran(src + s * 136 + dd);
      ml = ld_gran(src + (lane & 7) * 136 + 128 + ((lane >> 3) & 1));
      bool ok = (unsigned)(ml >> 32) == tag;
#pragma unroll
      for (int s = 0; s < 8; ++s) ok &= (unsigned)(a[s] >> 32) == tag;
      if (__all(ok)) break;
      __builtin_amdgcn_s_sleep(1);
    }
    const int mlv = (int)(unsigned)ml;
    float ms[8], M = -INFINITY;
#pragma unroll
    for (int s = 0; s < 8; ++s) { ms[s] = __int_as_float(__builtin_amdgcn_readlane(mlv, s)); M = fmaxf(M, ms[s]); }
    float L = 0.f, o = 0.f;
#pragma unroll
    for (int s = 0; s < 8; ++s) { const float wt = __builtin_amdgcn_exp2f(ms[s] - M);
      L = fmaf(wt, __int_as_float(__builtin_amdgcn_readlane(mlv, 8 + s)), L); o = fmaf(wt, __uint_as_float((unsigned)a[s]), o); }
    reinterpret_cast<u16*>(ldsu + L_O)[b * 128 + dd] = f2bf(o / L);
    bar();
    f32x4 acc[5];
#pragma unroll
    for (int ct = 0; ct < 5; ++ct) acc[ct] = f32x4{0.f, 0.f, 0.f, 0.f};
#pragma unroll
    for (int ks = 0; ks < 4; ++ks) {
      const u32x4 af = *reinterpret_cast<const u32x4*>(ldsu + L_O + (lane & 1) * 64 + ks * 16 + (lane >> 4) * 4);
#pragma unroll
      for (int ct = 0; ct < 5; ++ct) acc[ct] = __builtin_amdgcn_mfma_f32_16x16x32_bf16(__builtin_bit_cast(bf16x8, af), __builtin_bit_cast(bf16x8, Bf[ct][ks]), acc[ct], 0, 0, 0);
    }
    {
      const int r = lane >> 4;
      const float y0 = r == 0 ? acc[0][0] : r == 1 ? acc[1][0] : r == 2 ? acc[2][0] : acc[3][0];
      const float y1 = r == 0 ? acc[0][1] : r == 1 ? acc[1][1] : r == 2 ? acc[2][1] : acc[3][1];
      st_gran(dst + r * 16, tag, __float_as_uint(y0)); st_gran(dst + 32 * DM + r * 16, tag, __float_as_uint(y1));
      if (nt == 5 && lane < 32) st_gran(dst + (lane >> 4) * 32 * DM + 64, tag, __float_as_uint(lane < 16 ? acc[4][0] : acc[4][1]));
    }
  } else {
    const int job = wave * 128 + (bx >> 1);
    const int mb = job / 72, c0 = (job % 72) * 8;
    gu64* src = YG + (size_t)(mb * 32 + (lane >> 3)) * DM + c0 + (lane & 7);
    bar();
    if (job < 288) {
      u64 a[4];
      for (unsigned spin = 0; spin < SPIN_CAP; ++spin) {
#pragma unroll
        for (int i = 0; i < 4; ++i) a[i] = ld_gran(src + 8 * i * DM);
        bool ok = true;
#pragma unroll
        for (int i = 0; i < 4; ++i) ok &= (unsigned)(a[i] >> 32) == tag;
        if (__all(ok)) break;
        __builtin_amdgcn_s_sleep(1);
      }
      float v = (__uint_as_float((unsigned)a[0]) + __uint_as_float((unsigned)a[1])) + (__uint_as_float((unsigned)a[2]) + __uint_as_float((unsigned)a[3]));
      v += dpp<XOR8>(v);
      v = xrow_sum(v);
      if (lane < 8) p.out[(mb & 1) * 2 * DM + (mb >> 1) * DM + c0 + lane] = f2bf(v);
      if (job == 0 && lane == 0) __hip_atomic_store((gu32*)(p.ws + WS_EPOCH), tag, __ATOMIC_RELAXED, __HIP_MEMORY_SCOPE_AGENT);
    }
  }
}
extern "C" void kernel_launch(void* const* d_in, const int* in_sizes, int n_in, void* d_out, int out_size, void* d_ws, size_t ws_size, hipStream_t stream) {
  Params p;
  p.xA = (const u16*)d_in[0]; p.kA = (const u16*)d_in[1]; p.vA = (const u16*)d_in[2]; p.wqA = (const u16*)d_in[3]; p.woA = (const u16*)d_in[4];
  p.xB = (const u16*)d_in[5]; p.kB = (const u16*)d_in[6]; p.vB = (const u16*)d_in[7]; p.wqB = (const u16*)d_in[8]; p.woB = (const u16*)d_in[9];
  p.out = (u16*)d_out; p.ws = (unsigned char*)d_ws;
  k_fused<<<256, 1024, 0, stream>>>(p);
}
```
